```python
import torch

def _softmax(x, axis=-1):
    return torch.softmax(x, dim=axis)
def _gelu(x):
    return torch.nn.functional.gelu(x, approximate="tanh")
def _silu(x):
    return torch.nn.functional.silu(x)
def _sigmoid(x):
    return torch.sigmoid(x)
def _layernorm(x, axis=-1, eps=1e-5):
    m = x.mean(dim=axis, keepdim=True)
    v = x.var(dim=axis, keepdim=True, unbiased=False)
    return (x - m) / torch.sqrt(v + eps)
def _scatter_add(tgt, idx, src, axis=0):
    return tgt.clone().index_add_(axis, idx, src)
def _segment_sum(src, seg, n):
    out = torch.zeros((n,) + src.shape[1:], dtype=src.dtype)
    return out.index_add_(0, seg, src)
def _pad(x, pw):
    flat = tuple(v for pair in reversed(pw) for v in pair)
    return torch.nn.functional.pad(x, flat)

kernel_name = 'gather_scatter__cheap_equivariant_egnn__graph'

def setup_inputs():

    _w = (lambda a: (lambda t: t.to(torch.bfloat16) if t.is_floating_point() else t)(torch.as_tensor(a)))
    N, deg, D, De = 10000, 16, 256, max(0, 1)
    E = N * deg
    return {
        "h": _w((lambda *s: torch.randn(*s, dtype=torch.bfloat16) * 0.2)(N, D)),
        "pos": _w((lambda *s: torch.randn(*s, dtype=torch.bfloat16) * 0.8)(N, 3)),
        "e_attr": _w((lambda *s: torch.randn(*s, dtype=torch.bfloat16) * 0.8)(E, De)),
        "src": _w((lambda lo, hi, *s: torch.randint(lo, hi, s))(0, N, E).to(torch.int64)),
        "dst": _w((lambda lo, hi, *s: torch.randint(lo, hi, s))(0, N, E).to(torch.int64)),
        "w_e": _w(0.1 * (lambda *s: torch.randn(*s, dtype=torch.bfloat16) * 0.8)(2 * D + 1 + De, D)),
        "w_x": _w(0.1 * (lambda *s: torch.randn(*s, dtype=torch.bfloat16) * 0.2)(D, 1)),
        "w_h": _w(0.1 * (lambda *s: torch.randn(*s, dtype=torch.bfloat16) * 0.2)(2 * D, D)),
    }

def reference(h, pos, e_attr, src, dst, w_e, w_x, w_h):
    N, D = h.shape[0], h.shape[1]
    diff = pos[dst] - pos[src]
    dist2 = torch.sum(diff * diff, axis=-1, keepdims=True)
    e_in = torch.concatenate([h[dst], h[src], dist2, e_attr], axis=-1)
    m_ij = _silu(e_in @ w_e)
    coord_scale = m_ij @ w_x
    pos_new = pos + _segment_sum(diff * coord_scale, dst, N)
    m_i = _segment_sum(m_ij, dst, N)
    h_new = h + _silu(torch.concatenate([h, m_i], axis=-1) @ w_h)
    return h_new, pos_new
```

```cpp
#include <hip/hip_runtime.h>
#include <hip/hip_bf16.h>
#include <stdint.h>

typedef unsigned short u16;
typedef __attribute__((ext_vector_type(8))) short bf16x8;
typedef __attribute__((ext_vector_type(4))) short s16x4;
typedef __attribute__((ext_vector_type(4))) float f32x4;

constexpr int D = 256;
constexpr int TILE = 40;
constexpr int TROWS = 48;
constexpr int MAXC = 64;
constexpr int MAXE = 1024;
constexpr int HP = 264;
constexpr int FP = 260;

__device__ __forceinline__ float bf2f(u16 x) { return __uint_as_float(((unsigned)x) << 16); }
__device__ __forceinline__ u16 f2bf(float f) { return __builtin_bit_cast(u16, (__bf16)f); }
__device__ __forceinline__ unsigned pack2(float lo, float hi) { return (unsigned)f2bf(lo) | ((unsigned)f2bf(hi) << 16); }
__device__ __forceinline__ float rbf(float f) { return bf2f(f2bf(f)); }
__device__ __forceinline__ float silu_f(float x) {
  float t = __builtin_amdgcn_exp2f(-1.4426950408889634f * x);
  return x * __builtin_amdgcn_rcpf(1.0f + t);
}

__device__ __forceinline__ unsigned off_b(unsigned row, unsigned ch) {
  return 256u * row + 16u * (ch ^ (((row & 3) << 2) | ((row >> 2) & 3)));
}

__global__ __launch_bounds__(256) void k1_kernel(const u16* __restrict__ h, const u16* __restrict__ w_e,
                                                 const u16* __restrict__ w_h, const int* __restrict__ src,
                                                 const int* __restrict__ dst, const u16* __restrict__ eattr,
                                                 u16* __restrict__ Q, uint2* __restrict__ rec, int* __restrict__ off,
                                                 u16* __restrict__ WT, int N, int E, int C, int CH, int NBINS, int NG) {
  extern __shared__ __attribute__((aligned(16))) char smem[];
  const int tid = threadIdx.x, lane = tid & 63, w = tid >> 6;
  const int bid = blockIdx.x;
  if (bid < C) {
    int* hist = (int*)smem;
    int* wsum = hist + NBINS + 1;
    const int c = bid;
    const int e0 = c * CH, e1 = min(E, e0 + CH);
    for (int i = tid; i <= NBINS; i += 256) hist[i] = 0;
    __syncthreads();
    for (int e = e0 + tid; e < e1; e += 256) atomicAdd(&hist[(unsigned)dst[e] / TILE], 1);
    __syncthreads();
    const int per = (NBINS + 255) / 256;
    int s = 0;
    for (int i = 0; i < per; ++i) { int b = tid * per + i; if (b < NBINS) s += hist[b]; }
    int inc = s;
    for (int o = 1; o < 64; o <<= 1) { int t = __shfl_up(inc, o, 64); if (lane >= o) inc += t; }
    if (lane == 63) wsum[w] = inc;
    __syncthreads();
    int base = inc - s;
    for (int i = 0; i < w; ++i) base += wsum[i];
    for (int i = 0; i < per; ++i) {
      int b = tid * per + i;
      if (b < NBINS) { int t = hist[b]; hist[b] = base; off[b * C + c] = base; base += t; }
    }
    if (tid == 255) off[NBINS * C + c] = base;
    __syncthreads();
    uint2* out = rec + e0;
    for (int e = e0 + tid; e < e1; e += 256) {
      unsigned d = (unsigned)dst[e];
      unsigned b = d / TILE;
      int p = atomicAdd(&hist[b], 1);
      out[p] = make_uint2((unsigned)src[e], (d - b * TILE) | ((unsigned)eattr[e] << 16));
    }
  } else if (bid < C + NG) {
    const int g = bid - C;
    const int rb = g >> 1, cb = g & 1;
    const u16* We2 = w_e + D * D;
#pragma unroll
    for (int i = 0; i < 16; ++i) {
      int n = tid + 256 * i, row = n >> 4, ch = n & 15;
      *(uint4*)(smem + off_b(row, ch)) = *(const uint4*)(We2 + row * D + cb * 128 + ch * 8);
    }
    bf16x8 hf[4][8];
#pragma unroll
    for (int rt = 0; rt < 4; ++rt) {
      int row = min(rb * 64 + rt * 16 + (lane & 15), N - 1);
#pragma unroll
      for (int ks = 0; ks < 8; ++ks) hf[rt][ks] = *(const bf16x8*)(h + (size_t)row * D + ks * 32 + (lane >> 4) * 8);
    }
    __syncthreads();
    f32x4 acc[2][4];
#pragma unroll
    for (int a = 0; a < 2; ++a)
#pragma unroll
      for (int b = 0; b < 4; ++b) acc[a][b] = (f32x4){0.f, 0.f, 0.f, 0.f};
    const unsigned gq = lane >> 4, q = (lane & 15) >> 2, p = lane & 3;
#pragma unroll
    for (int ks = 0; ks < 8; ++ks) {
#pragma unroll
      for (int ci = 0; ci < 2; ++ci) {
        unsigned c = 2 * w + ci;
        typedef __attribute__((address_space(3))) s16x4* lp;
        s16x4 lo = __builtin_amdgcn_ds_read_tr16_b64_v4i16((lp)(smem + off_b(32 * ks + 8 * gq + q, 2 * c + (p >> 1)) + 8 * (p & 1)));
        s16x4 hi = __builtin_amdgcn_ds_read_tr16_b64_v4i16((lp)(smem + off_b(32 * ks + 8 * gq + 4 + q, 2 * c + (p >> 1)) + 8 * (p & 1)));
        bf16x8 wf = {lo[0], lo[1], lo[2], lo[3], hi[0], hi[1], hi[2], hi[3]};
#pragma unroll
        for (int rt = 0; rt < 4; ++rt) acc[ci][rt] = __builtin_amdgcn_mfma_f32_16x16x32_bf16(wf, hf[rt][ks], acc[ci][rt], 0, 0, 0);
      }
    }
#pragma unroll
    for (int rt = 0; rt < 4; ++rt) {
      int row = rb * 64 + rt * 16 + (lane & 15);
      if (row < N) {
#pragma unroll
        for (int ci = 0; ci < 2; ++ci) {
          uint2 v = make_uint2(pack2(acc[ci][rt][0], acc[ci][rt][1]), pack2(acc[ci][rt][2], acc[ci][rt][3]));
          *(uint2*)(Q + (size_t)row * D + cb * 128 + (2 * w + ci) * 16 + gq * 4) = v;
        }
      }
    }
  } else {
    const int t0 = (bid - C - NG) * 1024 + tid;
#pragma unroll
    for (int i = 0; i < 4; ++i) {
      int x = t0 + 256 * i;
      if (x < 3 * 8192) {
        int mat = x >> 13, r = x & 8191, ct = r >> 9, ks = (r >> 6) & 7, l = r & 63;
        const u16* W = mat == 0 ? w_e : (mat == 1 ? w_h : w_h + D * D);
        const u16* pw = W + (size_t)(32 * ks + 8 * (l >> 4)) * D + 16 * ct + (l & 15);
        unsigned v[4];
#pragma unroll
        for (int j = 0; j < 4; ++j) v[j] = (unsigned)pw[(2 * j) * D] | ((unsigned)pw[(2 * j + 1) * D] << 16);
        *(uint4*)(WT + (size_t)x * 8) = make_uint4(v[0], v[1], v[2], v[3]);
      }
    }
  }
}

struct K2Lds {
  u16 ht[TROWS * HP];
  float pt[TILE * FP];
  float mt[TILE * FP];
  float4 e4[MAXE];
  float ez[MAXE];
  int esrc[MAXE];
  u16 enode[MAXE];
  u16 eslot[MAXE];
  u16 perm[MAXE];
  int ncnt[TILE + 8];
  int nstart[TILE + 8];
  float cacc[TILE * 4];
};

__global__ __launch_bounds__(512) void k2_kernel(const u16* __restrict__ h, const u16* __restrict__ pos,
                                                 const u16* __restrict__ w_e, const u16* __restrict__ w_x,
                                                 const u16* __restrict__ Q, const uint2* __restrict__ rec,
                                                 const int* __restrict__ off, const u16* __restrict__ WT,
                                                 u16* __restrict__ out_h, u16* __restrict__ out_pos, int N, int C, int CH) {
  extern __shared__ __attribute__((aligned(16))) char smem[];
  K2Lds& S = *(K2Lds*)smem;
  const int tid = threadIdx.x, lane = tid & 63, w = tid >> 6;
  const int bin = blockIdx.x;
  const int node0 = bin * TILE;
  const int nvalid = min(TILE, N - node0);
  const int half = lane >> 5, cl = lane & 31;

  const bf16x8* WTf = (const bf16x8*)WT;
  bf16x8 wf1[2][8], wf2[2][8];
#pragma unroll
  for (int ci = 0; ci < 2; ++ci)
#pragma unroll
    for (int ks = 0; ks < 8; ++ks) {
      wf1[ci][ks] = WTf[0 * 8192 + ((2 * w + ci) * 8 + ks) * 64 + lane];
      wf2[ci][ks] = WTf[1 * 8192 + ((2 * w + ci) * 8 + ks) * 64 + lane];
    }
  int seg_s = 0, seg_n = 0;
  if (lane < C) { seg_s = off[bin * C + lane]; seg_n = off[(bin + 1) * C + lane] - seg_s; }
#pragma unroll
  for (int i = 0; i < 3; ++i) {
    int n = tid + 512 * i, row = n >> 5, ch = n & 31;
    int gr = min(node0 + row, N - 1);
    *(uint4*)(S.ht + row * HP + ch * 8) = *(const uint4*)(h + (size_t)gr * D + ch * 8);
  }
  int inc = seg_n;
  for (int o = 1; o < 64; o <<= 1) { int t = __shfl_up(inc, o, 64); if (lane >= o) inc += t; }
  const int seg_b = inc - seg_n;
  const int total = __shfl(inc, 63, 64);
  __syncthreads();

  f32x4 acc1[2][3];
  {
    f32x4 accp[2][3];
#pragma unroll
    for (int ci = 0; ci < 2; ++ci)
#pragma unroll
      for (int rt = 0; rt < 3; ++rt) { accp[ci][rt] = (f32x4){0.f, 0.f, 0.f, 0.f}; acc1[ci][rt] = (f32x4){0.f, 0.f, 0.f, 0.f}; }
#pragma unroll
    for (int ks = 0; ks < 8; ++ks)
#pragma unroll
      for (int rt = 0; rt < 3; ++rt) {
        bf16x8 hf = *(const bf16x8*)(S.ht + (rt * 16 + (lane & 15)) * HP + ks * 32 + (lane >> 4) * 8);
#pragma unroll
        for (int ci = 0; ci < 2; ++ci) {
          accp[ci][rt] = __builtin_amdgcn_mfma_f32_16x16x32_bf16(wf1[ci][ks], hf, accp[ci][rt], 0, 0, 0);
          acc1[ci][rt] = __builtin_amdgcn_mfma_f32_16x16x32_bf16(wf2[ci][ks], hf, acc1[ci][rt], 0, 0, 0);
        }
      }
#pragma unroll
    for (int rt = 0; rt < 3; ++rt) {
      int row = rt * 16 + (lane & 15);
      if (row < TILE) {
#pragma unroll
        for (int ci = 0; ci < 2; ++ci) *(f32x4*)(S.pt + row * FP + (2 * w + ci) * 16 + (lane >> 4) * 4) = accp[ci][rt];
      }
    }
  }

  float wd[8], wa[8], wx[8];
  {
    uint4 a = *(const uint4*)(w_e + 2 * D * D + cl * 8);
    uint4 b = *(const uint4*)(w_e + (2 * D + 1) * D + cl * 8);
    uint4 c = *(const uint4*)(w_x + cl * 8);
    unsigned aa[4] = {a.x, a.y, a.z, a.w}, bb[4] = {b.x, b.y, b.z, b.w}, cc[4] = {c.x, c.y, c.z, c.w};
#pragma unroll
    for (int j = 0; j < 4; ++j) {
      wd[2 * j] = __uint_as_float(aa[j] << 16); wd[2 * j + 1] = __uint_as_float(aa[j] & 0xffff0000u);
      wa[2 * j] = __uint_as_float(bb[j] << 16); wa[2 * j + 1] = __uint_as_float(bb[j] & 0xffff0000u);
      wx[2 * j] = __uint_as_float(cc[j] << 16); wx[2 * j + 1] = __uint_as_float(cc[j] & 0xffff0000u);
    }
  }
  float px = 0.f, py = 0.f, pz = 0.f;
  if (tid < nvalid) {
    px = bf2f(pos[(node0 + tid) * 3 + 0]); py = bf2f(pos[(node0 + tid) * 3 + 1]); pz = bf2f(pos[(node0 + tid) * 3 + 2]);
  }

  for (int b0 = 0; b0 == 0 || b0 < total; b0 += MAXE) {
    const int nb = min(MAXE, total - b0);
    if (tid < TILE + 8) S.ncnt[tid] = 0;
    __syncthreads();
    for (int c = w; c < C; c += 8) {
      int ss = __shfl(seg_s, c, 64), sn = __shfl(seg_n, c, 64), sb = __shfl(seg_b, c, 64);
      for (int j = lane; j < sn; j += 64) {
        int gp = sb + j - b0;
        if (gp >= 0 && gp < MAXE) {
          uint2 r = rec[(size_t)c * CH + ss + j];
          int nl = r.y & 0xffff;
          int s = (int)r.x;
          float sx = bf2f(pos[s * 3 + 0]), sy = bf2f(pos[s * 3 + 1]), sz = bf2f(pos[s * 3 + 2]);
          int dn = node0 + nl;
          float qx = bf2f(pos[dn * 3 + 0]), qy = bf2f(pos[dn * 3 + 1]), qz = bf2f(pos[dn * 3 + 2]);
          float dx = rbf(qx - sx), dy = rbf(qy - sy), dz = rbf(qz - sz);
          float d2 = rbf(rbf(dx * dx) + rbf(dy * dy) + rbf(dz * dz));
          S.e4[gp] = make_float4(d2, __uint_as_float(r.y & 0xffff0000u), dx, dy);
          S.ez[gp] = dz;
          S.esrc[gp] = s;
          S.enode[gp] = (u16)nl;
          S.eslot[gp] = (u16)atomicAdd(&S.ncnt[nl], 1);
        }
      }
    }
    __syncthreads();
    if (w == 0) {
      int cnt = lane < TILE ? S.ncnt[lane] : 0;
      int ic = cnt;
      for (int o = 1; o < 64; o <<= 1) { int t = __shfl_up(ic, o, 64); if (lane >= o) ic += t; }
      if (lane <= TILE) S.nstart[lane] = ic - cnt;
    }
    __syncthreads();
    for (int t = tid; t < nb; t += 512) S.perm[S.nstart[S.enode[t]] + S.eslot[t]] = (u16)t;
    __syncthreads();

    for (int nl = w; nl < nvalid; nl += 8) {
      const int k_beg = S.nstart[nl], k_end = S.nstart[nl + 1];
      float p[8];
      {
        float4 a = *(const float4*)(S.pt + nl * FP + cl * 8), b = *(const float4*)(S.pt + nl * FP + cl * 8 + 4);
        p[0] = a.x; p[1] = a.y; p[2] = a.z; p[3] = a.w; p[4] = b.x; p[5] = b.y; p[6] = b.z; p[7] = b.w;
      }
      float am[8], ax[8], ay[8], az[8];
#pragma unroll
      for (int j = 0; j < 8; ++j) { am[j] = 0.f; ax[j] = 0.f; ay[j] = 0.f; az[j] = 0.f; }
      for (int k0 = k_beg; k0 < k_end; k0 += 16) {
        uint4 q[8];
        int idx[8];
#pragma unroll
        for (int u = 0; u < 8; ++u) {
          if (k0 + 2 * u < k_end) {
            int e = k0 + 2 * u + half;
            idx[u] = S.perm[e < k_end ? e : k_beg];
            q[u] = *(const uint4*)(Q + (size_t)S.esrc[idx[u]] * D + cl * 8);
          }
        }
#pragma unroll
        for (int u = 0; u < 8; ++u) {
          if (k0 + 2 * u < k_end) {
            float4 s = S.e4[idx[u]];
            float dz = S.ez[idx[u]];
            if (k0 + 2 * u + half < k_end) {
              unsigned qq[4] = {q[u].x, q[u].y, q[u].z, q[u].w};
#pragma unroll
              for (int j = 0; j < 8; ++j) {
                float qv = (j & 1) ? __uint_as_float(qq[j >> 1] & 0xffff0000u) : __uint_as_float(qq[j >> 1] << 16);
                float x = fmaf(s.y, wa[j], fmaf(s.x, wd[j], p[j])) + qv;
                float m = silu_f(x);
                am[j] += m;
                ax[j] = fmaf(s.z, m, ax[j]);
                ay[j] = fmaf(s.w, m, ay[j]);
                az[j] = fmaf(dz, m, az[j]);
              }
            }
          }
        }
      }
      float cx = 0.f, cy = 0.f, cz = 0.f;
#pragma unroll
      for (int j = 0; j < 8; ++j) {
        am[j] += __shfl_xor(am[j], 32, 64);
        cx = fmaf(wx[j], ax[j], cx); cy = fmaf(wx[j], ay[j], cy); cz = fmaf(wx[j], az[j], cz);
      }
      for (int o = 32; o >= 1; o >>= 1) {
        cx += __shfl_xor(cx, o, 64); cy += __shfl_xor(cy, o, 64); cz += __shfl_xor(cz, o, 64);
      }
      if (half == 0) {
        float4* mp = (float4*)(S.mt + nl * FP + cl * 8);
        float4 a = make_float4(am[0], am[1], am[2], am[3]), b = make_float4(am[4], am[5], am[6], am[7]);
        if (b0 > 0) {
          float4 oa = mp[0], ob = mp[1];
          a.x += oa.x; a.y += oa.y; a.z += oa.z; a.w += oa.w; b.x += ob.x; b.y += ob.y; b.z += ob.z; b.w += ob.w;
        }
        mp[0] = a; mp[1] = b;
        if (lane == 0) {
          if (b0 > 0) { cx += S.cacc[nl * 4 + 0]; cy += S.cacc[nl * 4 + 1]; cz += S.cacc[nl * 4 + 2]; }
          S.cacc[nl * 4 + 0] = cx; S.cacc[nl * 4 + 1] = cy; S.cacc[nl * 4 + 2] = cz;
        }
      }
    }
    __syncthreads();
  }

  __builtin_amdgcn_sched_barrier(0);
  bf16x8 wf3[2][8];
#pragma unroll
  for (int ci = 0; ci < 2; ++ci)
#pragma unroll
    for (int ks = 0; ks < 8; ++ks) wf3[ci][ks] = WTf[2 * 8192 + ((2 * w + ci) * 8 + ks) * 64 + lane];
#pragma unroll
  for (int ks = 0; ks < 8; ++ks)
#pragma unroll
    for (int rt = 0; rt < 3; ++rt) {
      int row = min(rt * 16 + (lane & 15), TILE - 1);
      const float* mp = S.mt + row * FP + ks * 32 + (lane >> 4) * 8;
      float4 a = *(const float4*)mp, b = *(const float4*)(mp + 4);
      uint4 pk = make_uint4(pack2(a.x, a.y), pack2(a.z, a.w), pack2(b.x, b.y), pack2(b.z, b.w));
      bf16x8 mf = __builtin_bit_cast(bf16x8, pk);
#pragma unroll
      for (int ci = 0; ci < 2; ++ci) acc1[ci][rt] = __builtin_amdgcn_mfma_f32_16x16x32_bf16(wf3[ci][ks], mf, acc1[ci][rt], 0, 0, 0);
    }
#pragma unroll
  for (int rt = 0; rt < 3; ++rt) {
    int row = rt * 16 + (lane & 15);
    if (row < nvalid) {
#pragma unroll
      for (int ci = 0; ci < 2; ++ci) {
        int col = (2 * w + ci) * 16 + (lane >> 4) * 4;
        uint2 hv = *(const uint2*)(S.ht + row * HP + col);
        float o0 = __uint_as_float(hv.x << 16) + silu_f(acc1[ci][rt][0]);
        float o1 = __uint_as_float(hv.x & 0xffff0000u) + silu_f(acc1[ci][rt][1]);
        float o2 = __uint_as_float(hv.y << 16) + silu_f(acc1[ci][rt][2]);
        float o3 = __uint_as_float(hv.y & 0xffff0000u) + silu_f(acc1[ci][rt][3]);
        *(uint2*)(out_h + (size_t)(node0 + row) * D + col) = make_uint2(pack2(o0, o1), pack2(o2, o3));
      }
    }
  }
  if (tid < nvalid) {
    out_pos[(node0 + tid) * 3 + 0] = f2bf(px + S.cacc[tid * 4 + 0]);
    out_pos[(node0 + tid) * 3 + 1] = f2bf(py + S.cacc[tid * 4 + 1]);
    out_pos[(node0 + tid) * 3 + 2] = f2bf(pz + S.cacc[tid * 4 + 2]);
  }
}

extern "C" void kernel_launch(void* const* d_in, const int* in_sizes, int n_in, void* d_out, int out_size, void* d_ws,
                              size_t ws_size, hipStream_t stream) {
  const u16* h = (const u16*)d_in[0];
  const u16* pos = (const u16*)d_in[1];
  const u16* eattr = (const u16*)d_in[2];
  const int* src = (const int*)d_in[3];
  const int* dst = (const int*)d_in[4];
  const u16* w_e = (const u16*)d_in[5];
  const u16* w_x = (const u16*)d_in[6];
  const u16* w_h = (const u16*)d_in[7];
  const int N = in_sizes[0] / D;
  const int E = in_sizes[3];
  u16* out_h = (u16*)d_out;
  u16* out_pos = out_h + (size_t)N * D;

  const int NBINS = (N + TILE - 1) / TILE;
  const int C = MAXC;
  const int CH = (E + C - 1) / C;
  char* ws = (char*)d_ws;
  size_t o = 0;
  auto take = [&](size_t bytes) { char* p = ws + o; o += (bytes + 255) & ~(size_t)255; return p; };
  u16* Q = (u16*)take((size_t)N * D * 2);
  uint2* rec = (uint2*)take((size_t)C * CH * 8);
  int* off = (int*)take((size_t)(NBINS + 1) * C * 4);
  u16* WT = (u16*)take((size_t)3 * 8192 * 16);

  const int NG = ((N + 63) / 64) * 2;
  const int NT = 24;
  const size_t lds1 = 65536 > (size_t)(NBINS + 8) * 4 ? 65536 : (size_t)(NBINS + 8) * 4;
  k1_kernel<<<C + NG + NT, 256, lds1, stream>>>(h, w_e, w_h, src, dst, eattr, Q, rec, off, WT, N, E, C, CH, NBINS, NG);
  hipFuncSetAttribute((const void*)k2_kernel, hipFuncAttributeMaxDynamicSharedMemorySize, (int)sizeof(K2Lds));
  k2_kernel<<<NBINS, 512, sizeof(K2Lds), stream>>>(h, pos, w_e, w_x, Q, rec, off, WT, out_h, out_pos, N, C, CH);
}
```

```cpp
#include <hip/hip_runtime.h>
#include <hip/hip_bf16.h>
#include <stdint.h>

typedef unsigned short u16;
typedef __attribute__((ext_vector_type(8))) short bf16x8;
typedef __attribute__((ext_vector_type(4))) short s16x4;
typedef __attribute__((ext_vector_type(4))) float f32x4;

constexpr int D = 256;
constexpr int TILE = 40;
constexpr int TROWS = 48;
constexpr int MAXC = 256;
constexpr int MAXE = 1024;
constexpr int HP = 264;
constexpr int FP = 260;

__device__ __forceinline__ float bf2f(u16 x) { return __uint_as_float(((unsigned)x) << 16); }
__device__ __forceinline__ u16 f2bf(float f) { return __builtin_bit_cast(u16, (__bf16)f); }
__device__ __forceinline__ unsigned pack2(float lo, float hi) { return (unsigned)f2bf(lo) | ((unsigned)f2bf(hi) << 16); }
__device__ __forceinline__ float rbf(float f) { return bf2f(f2bf(f)); }
__device__ __forceinline__ float silu_f(float x) {
  float t = __builtin_amdgcn_exp2f(-1.4426950408889634f * x);
  return x * __builtin_amdgcn_rcpf(1.0f + t);
}

__device__ __forceinline__ unsigned off_b(unsigned row, unsigned ch) {
  return 128u * row + 16u * (ch ^ ((((row >> 1) & 1) | (((row >> 3) & 1) << 1)) << 1));
}

__global__ __launch_bounds__(256) void k1_kernel(const u16* __restrict__ h, const u16* __restrict__ w_e,
                                                 const u16* __restrict__ w_h, const int* __restrict__ src,
                                                 const int* __restrict__ dst, const u16* __restrict__ eattr,
                                                 const u16* __restrict__ pos, u16* __restrict__ Q, uint4* __restrict__ rec,
                                                 int* __restrict__ off, u16* __restrict__ WT, int N, int E, int C, int CH,
                                                 int NBINS, int NG) {
  extern __shared__ __attribute__((aligned(16))) char smem[];
  const int tid = threadIdx.x, lane = tid & 63, w = tid >> 6;
  const int bid = blockIdx.x;
  if (bid < C) {
    int* hist = (int*)smem;
    int* wsum = hist + NBINS + 1;
    const int c = bid;
    const int e0 = c * CH, e1 = min(E, e0 + CH);
    for (int i = tid; i <= NBINS; i += 256) hist[i] = 0;
    __syncthreads();
    for (int eb = e0; eb < e1; eb += 2560) {
      unsigned dv[10];
#pragma unroll
      for (int i = 0; i < 10; ++i) { int e = eb + tid + 256 * i; dv[i] = e < e1 ? (unsigned)dst[e] : 0xffffffffu; }
#pragma unroll
      for (int i = 0; i < 10; ++i) if (dv[i] != 0xffffffffu) atomicAdd(&hist[dv[i] / TILE], 1);
    }
    __syncthreads();
    const int per = (NBINS + 255) / 256;
    int s = 0;
    for (int i = 0; i < per; ++i) { int b = tid * per + i; if (b < NBINS) s += hist[b]; }
    int inc = s;
    for (int o = 1; o < 64; o <<= 1) { int t = __shfl_up(inc, o, 64); if (lane >= o) inc += t; }
    if (lane == 63) wsum[w] = inc;
    __syncthreads();
    int base = inc - s;
    for (int i = 0; i < w; ++i) base += wsum[i];
    for (int i = 0; i < per; ++i) {
      int b = tid * per + i;
      if (b < NBINS) { int t = hist[b]; hist[b] = base; off[b * C + c] = base; base += t; }
    }
    if (tid == 255) off[NBINS * C + c] = base;
    __syncthreads();
    uint4* out = rec + e0;
    for (int eb = e0; eb < e1; eb += 2560) {
      unsigned dv[10], sv[10], av[10];
#pragma unroll
      for (int i = 0; i < 10; ++i) {
        int e = eb + tid + 256 * i;
        bool ok = e < e1;
        dv[i] = ok ? (unsigned)dst[e] : 0xffffffffu; sv[i] = ok ? (unsigned)src[e] : 0u; av[i] = ok ? (unsigned)eattr[e] : 0u;
      }
      u16 ps[10][3], pd[10][3];
#pragma unroll
      for (int i = 0; i < 10; ++i) {
        unsigned d = dv[i] == 0xffffffffu ? 0u : dv[i];
#pragma unroll
        for (int k = 0; k < 3; ++k) { ps[i][k] = pos[sv[i] * 3 + k]; pd[i][k] = pos[d * 3 + k]; }
      }
#pragma unroll
      for (int i = 0; i < 10; ++i) if (dv[i] != 0xffffffffu) {
        unsigned b = dv[i] / TILE;
        int p = atomicAdd(&hist[b], 1);
        float dx = rbf(bf2f(pd[i][0]) - bf2f(ps[i][0])), dy = rbf(bf2f(pd[i][1]) - bf2f(ps[i][1])), dz = rbf(bf2f(pd[i][2]) - bf2f(ps[i][2]));
        float d2 = rbf(dx * dx) + rbf(dy * dy) + rbf(dz * dz);
        out[p] = make_uint4(sv[i], (dv[i] - b * TILE) | (av[i] << 16), (unsigned)f2bf(dx) | ((unsigned)f2bf(dy) << 16),
                            (unsigned)f2bf(dz) | ((unsigned)f2bf(d2) << 16));
      }
    }
  } else if (bid < C + NG) {
    const int g = bid - C;
    const int rb = g >> 2, cb = g & 3;
    const u16* We2 = w_e + D * D;
#pragma unroll
    for (int i = 0; i < 8; ++i) {
      int n = tid + 256 * i, row = n >> 3, ch = n & 7;
      *(uint4*)(smem + off_b(row, ch)) = *(const uint4*)(We2 + row * D + cb * 64 + ch * 8);
    }
    bf16x8 hf[8];
    const int myrow = rb * 64 + w * 16 + (lane & 15);
    {
      int row = min(myrow, N - 1);
#pragma unroll
      for (int ks = 0; ks < 8; ++ks) hf[ks] = *(const bf16x8*)(h + (size_t)row * D + ks * 32 + (lane >> 4) * 8);
    }
    __syncthreads();
    f32x4 acc[4];
#pragma unroll
    for (int a = 0; a < 4; ++a) acc[a] = (f32x4){0.f, 0.f, 0.f, 0.f};
    const unsigned gq = lane >> 4, q = (lane & 15) >> 2, p = lane & 3;
#pragma unroll
    for (int ks = 0; ks < 8; ++ks) {
#pragma unroll
      for (int c = 0; c < 4; ++c) {
        typedef __attribute__((address_space(3))) s16x4* lp;
        s16x4 lo = __builtin_amdgcn_ds_read_tr16_b64_v4i16((lp)(smem + off_b(32 * ks + 8 * gq + q, 2 * c + (p >> 1)) + 8 * (p & 1)));
        s16x4 hi = __builtin_amdgcn_ds_read_tr16_b64_v4i16((lp)(smem + off_b(32 * ks + 8 * gq + 4 + q, 2 * c + (p >> 1)) + 8 * (p & 1)));
        bf16x8 wf = {lo[0], lo[1], lo[2], lo[3], hi[0], hi[1], hi[2], hi[3]};
        acc[c] = __builtin_amdgcn_mfma_f32_16x16x32_bf16(wf, hf[ks], acc[c], 0, 0, 0);
      }
    }
    if (myrow < N) {
      const float sc = -1.4426950408889634f;
#pragma unroll
      for (int c = 0; c < 4; ++c) {
        uint2 v = make_uint2(pack2(acc[c][0] * sc, acc[c][1] * sc), pack2(acc[c][2] * sc, acc[c][3] * sc));
        *(uint2*)(Q + (size_t)myrow * D + cb * 64 + c * 16 + gq * 4) = v;
      }
    }
  } else {
    const int t0 = (bid - C - NG) * 1024 + tid;
#pragma unroll
    for (int i = 0; i < 4; ++i) {
      int x = t0 + 256 * i;
      if (x < 3 * 8192) {
        int mat = x >> 13, r = x & 8191, ct = r >> 9, ks = (r >> 6) & 7, l = r & 63;
        const u16* W = mat == 0 ? w_e : (mat == 1 ? w_h : w_h + D * D);
        const u16* pw = W + (size_t)(32 * ks + 8 * (l >> 4)) * D + 16 * ct + (l & 15);
        unsigned v[4];
#pragma unroll
        for (int j = 0; j < 4; ++j) v[j] = (unsigned)pw[(2 * j) * D] | ((unsigned)pw[(2 * j + 1) * D] << 16);
        *(uint4*)(WT + (size_t)x * 8) = make_uint4(v[0], v[1], v[2], v[3]);
      }
    }
  }
}

struct K2Lds {
  u16 ht[TROWS * HP];
  float pt[TILE * FP];
  float mt[TILE * FP];
  float4 srt4[MAXE];
  float4 srtd[MAXE];
  int ncnt[TILE + 8];
  int nstart[TILE + 8];
  float cacc[TILE * 4];
  int seg_s[MAXC], seg_b[MAXC + 1];
};
typedef __attribute__((ext_vector_type(2))) float f32x2;
#define NLOG2E (-1.4426950408889634f)

__global__ __launch_bounds__(1024) void k2_kernel(const u16* __restrict__ h, const u16* __restrict__ pos,
                                                  const u16* __restrict__ w_e, const u16* __restrict__ w_x,
                                                  const u16* __restrict__ Q, const uint4* __restrict__ rec,
                                                  const int* __restrict__ off, const u16* __restrict__ WT,
                                                  u16* __restrict__ out_h, u16* __restrict__ out_pos, int N, int C, int CH) {
  extern __shared__ __attribute__((aligned(16))) char smem[];
  K2Lds& S = *(K2Lds*)smem;
  const int tid = threadIdx.x, lane = tid & 63, w = tid >> 6;
  const int bin = blockIdx.x;
#define STAMP(i)
  const int node0 = bin * TILE;
  const int nvalid = min(TILE, N - node0);
  const int half = lane >> 5, cl = lane & 31;

  const bf16x8* WTf = (const bf16x8*)WT;
  if (w == 1) {
    int ss[4], sn[4], tot = 0;
#pragma unroll
    for (int i = 0; i < 4; ++i) {
      int c = lane * 4 + i;
      ss[i] = 0; sn[i] = 0;
      if (c < C) { ss[i] = off[bin * C + c]; sn[i] = off[(bin + 1) * C + c] - ss[i]; }
      tot += sn[i];
    }
    int inc = tot;
    for (int o = 1; o < 64; o <<= 1) { int t = __shfl_up(inc, o, 64); if (lane >= o) inc += t; }
    int b = inc - tot;
#pragma unroll
    for (int i = 0; i < 4; ++i) { S.seg_s[lane * 4 + i] = ss[i]; S.seg_b[lane * 4 + i] = b; b += sn[i]; }
    if (lane == 63) S.seg_b[256] = inc;
  }
#pragma unroll
  for (int i = 0; i < 2; ++i) {
    int n = tid + 1024 * i, row = n >> 5, ch = n & 31;
    if (n < TROWS * 32) {
      int gr = min(node0 + row, N - 1);
      *(uint4*)(S.ht + row * HP + ch * 8) = *(const uint4*)(h + (size_t)gr * D + ch * 8);
    }
  }
  {
    bf16x8 wf1[8];
#pragma unroll
    for (int ks = 0; ks < 8; ++ks) wf1[ks] = WTf[0 * 8192 + (w * 8 + ks) * 64 + lane];
    if (tid < TILE + 8) S.ncnt[tid] = 0;
    __syncthreads();
    const int total = S.seg_b[256];
    uint4 r = make_uint4(0, 0, 0, 0);
    const bool rv = tid < total;
    if (rv) {
      int lo = 0, hi = 255;
      while (lo < hi) { int mid = (lo + hi + 1) >> 1; if (S.seg_b[mid] <= tid) lo = mid; else hi = mid - 1; }
      r = rec[(size_t)lo * CH + S.seg_s[lo] + (tid - S.seg_b[lo])];
    }
    f32x4 accp[3];
#pragma unroll
    for (int rt = 0; rt < 3; ++rt) accp[rt] = (f32x4){0.f, 0.f, 0.f, 0.f};
#pragma unroll
    for (int ks = 0; ks < 8; ++ks)
#pragma unroll
      for (int rt = 0; rt < 3; ++rt) {
        bf16x8 hf = *(const bf16x8*)(S.ht + (rt * 16 + (lane & 15)) * HP + ks * 32 + (lane >> 4) * 8);
        accp[rt] = __builtin_amdgcn_mfma_f32_16x16x32_bf16(wf1[ks], hf, accp[rt], 0, 0, 0);
      }
#pragma unroll
    for (int rt = 0; rt < 3; ++rt) {
      int row = rt * 16 + (lane & 15);
      if (row < TILE) *(f32x4*)(S.pt + row * FP + w * 16 + (lane >> 4) * 4) = accp[rt] * NLOG2E;
    }
    STAMP(0);
    for (int b0 = 0; b0 == 0 || b0 < total; b0 += MAXE) {
      bool v = rv;
      const int nb = min(MAXE, total - b0);
      if (b0 > 0) {
        if (tid < TILE + 8) S.ncnt[tid] = 0;
        __syncthreads();
        int gp = b0 + tid;
        v = gp < total;
        if (v) {
          int lo = 0, hi = 255;
          while (lo < hi) { int mid = (lo + hi + 1) >> 1; if (S.seg_b[mid] <= gp) lo = mid; else hi = mid - 1; }
          r = rec[(size_t)lo * CH + S.seg_s[lo] + (gp - S.seg_b[lo])];
        }
      }
      int slot = 0;
      if (v) slot = atomicAdd(&S.ncnt[r.y & 0xffff], 1);
      STAMP(1);
      __syncthreads();
      if (w == 0) {
        int cnt = lane < TILE ? S.ncnt[lane] : 0;
        int ic = cnt;
        for (int o = 1; o < 64; o <<= 1) { int t = __shfl_up(ic, o, 64); if (lane >= o) ic += t; }
        if (lane <= TILE) S.nstart[lane] = ic - cnt;
      }
      __syncthreads();
      if (v) {
        int k = S.nstart[r.y & 0xffff] + slot;
        S.srt4[k] = make_float4(__uint_as_float(r.x), __uint_as_float(r.w & 0xffff0000u), __uint_as_float(r.y & 0xffff0000u),
                                __uint_as_float(r.y & 0xffffu));
        S.srtd[k] = make_float4(__uint_as_float(r.z << 16), __uint_as_float(r.z & 0xffff0000u), __uint_as_float(r.w << 16), 0.f);
      }
      __syncthreads();
      STAMP(3);

      f32x2 wd[2], wa[2], wx[2];
      {
        uint2 a = *(const uint2*)(w_e + 2 * D * D + lane * 4);
        uint2 b = *(const uint2*)(w_e + (2 * D + 1) * D + lane * 4);
        uint2 c = *(const uint2*)(w_x + lane * 4);
        wd[0] = (f32x2){__uint_as_float(a.x << 16) * NLOG2E, __uint_as_float(a.x & 0xffff0000u) * NLOG2E};
        wd[1] = (f32x2){__uint_as_float(a.y << 16) * NLOG2E, __uint_as_float(a.y & 0xffff0000u) * NLOG2E};
        wa[0] = (f32x2){__uint_as_float(b.x << 16) * NLOG2E, __uint_as_float(b.x & 0xffff0000u) * NLOG2E};
        wa[1] = (f32x2){__uint_as_float(b.y << 16) * NLOG2E, __uint_as_float(b.y & 0xffff0000u) * NLOG2E};
        wx[0] = (f32x2){__uint_as_float(c.x << 16), __uint_as_float(c.x & 0xffff0000u)};
        wx[1] = (f32x2){__uint_as_float(c.y << 16), __uint_as_float(c.y & 0xffff0000u)};
      }
      for (int nl = w; nl < nvalid; nl += 16) {
        const int k_beg = S.nstart[nl], k_end = S.nstart[nl + 1];
        f32x2 p[2], am[2];
        float cx = 0.f, cy = 0.f, cz = 0.f;
        {
          float4 a = *(const float4*)(S.pt + nl * FP + lane * 4);
          p[0] = (f32x2){a.x, a.y}; p[1] = (f32x2){a.z, a.w};
        }
        am[0] = (f32x2){0.f, 0.f}; am[1] = (f32x2){0.f, 0.f};
        for (int k0 = k_beg; k0 < k_end; k0 += 16) {
          uint2 q[16];
#pragma unroll
          for (int u = 0; u < 16; ++u) {
            if (k0 + u < k_end) q[u] = *(const uint2*)((const char*)Q + (__float_as_uint(S.srt4[k0 + u].x) * (unsigned)(D * 2) + (unsigned)lane * 8u));
          }
          __builtin_amdgcn_sched_barrier(0);
#pragma unroll
          for (int u = 0; u < 16; ++u) {
            if (k0 + u < k_end) {
              float4 s = S.srt4[k0 + u];
              float4 dd = S.srtd[k0 + u];
              f32x2 sc2 = {0.f, 0.f};
#pragma unroll
              for (int j = 0; j < 2; ++j) {
                unsigned qq = j ? q[u].y : q[u].x;
                f32x2 qv = {__uint_as_float(qq << 16), __uint_as_float(qq & 0xffff0000u)};
                f32x2 x = __builtin_elementwise_fma(wa[j], (f32x2){s.z, s.z}, __builtin_elementwise_fma(wd[j], (f32x2){s.y, s.y}, p[j])) + qv;
                f32x2 t = {__builtin_amdgcn_exp2f(x.x), __builtin_amdgcn_exp2f(x.y)};
                t = t + (f32x2){1.f, 1.f};
                f32x2 rr = {__builtin_amdgcn_rcpf(t.x), __builtin_amdgcn_rcpf(t.y)};
                f32x2 m = x * rr;
                am[j] += m;
                sc2 = __builtin_elementwise_fma(wx[j], m, sc2);
              }
              float sc = sc2.x + sc2.y;
              cx = fmaf(dd.x, sc, cx); cy = fmaf(dd.y, sc, cy); cz = fmaf(dd.z, sc, cz);
            }
            __builtin_amdgcn_sched_barrier(0);
          }
        }
        for (int o = 32; o >= 1; o >>= 1) {
          cx += __shfl_xor(cx, o, 64); cy += __shfl_xor(cy, o, 64); cz += __shfl_xor(cz, o, 64);
        }
        const float LN2N = -0.6931471805599453f;
        {
          float4* mp = (float4*)(S.mt + nl * FP + lane * 4);
          float4 a = make_float4(am[0].x * LN2N, am[0].y * LN2N, am[1].x * LN2N, am[1].y * LN2N);
          if (b0 > 0) { float4 oa = *mp; a.x += oa.x; a.y += oa.y; a.z += oa.z; a.w += oa.w; }
          if (b0 + MAXE < total) *mp = a;
          else
            *(uint2*)((char*)(S.mt + nl * FP) + lane * 8) = make_uint2(pack2(a.x, a.y), pack2(a.z, a.w));
          if (lane == 0) {
            cx *= LN2N; cy *= LN2N; cz *= LN2N;
            if (b0 > 0) { cx += S.cacc[nl * 4 + 0]; cy += S.cacc[nl * 4 + 1]; cz += S.cacc[nl * 4 + 2]; }
            S.cacc[nl * 4 + 0] = cx; S.cacc[nl * 4 + 1] = cy; S.cacc[nl * 4 + 2] = cz;
          }
        }
      }
      STAMP(4);
      if (b0 + MAXE < total) __syncthreads();
    }
  }

  {
  int tid2 = threadIdx.x;
  asm volatile("" : "+v"(tid2));
  const int lane2 = tid2 & 63, w2 = __builtin_amdgcn_readfirstlane(tid2 >> 6);
  f32x4 acc1[3];
#pragma unroll
  for (int rt = 0; rt < 3; ++rt) acc1[rt] = (f32x4){0.f, 0.f, 0.f, 0.f};
  {
    bf16x8 wf2[8], wf3[8];
#pragma unroll
    for (int ks = 0; ks < 8; ++ks) {
      wf2[ks] = WTf[1 * 8192 + (w2 * 8 + ks) * 64 + lane2];
      wf3[ks] = WTf[2 * 8192 + (w2 * 8 + ks) * 64 + lane2];
    }
    __syncthreads();
    STAMP(5);
#pragma unroll
    for (int ks = 0; ks < 8; ++ks)
#pragma unroll
      for (int rt = 0; rt < 3; ++rt) {
        bf16x8 hf = *(const bf16x8*)(S.ht + (rt * 16 + (lane2 & 15)) * HP + ks * 32 + (lane2 >> 4) * 8);
        acc1[rt] = __builtin_amdgcn_mfma_f32_16x16x32_bf16(wf2[ks], hf, acc1[rt], 0, 0, 0);
        int row = min(rt * 16 + (lane2 & 15), TILE - 1);
        bf16x8 mf = *(const bf16x8*)((const char*)(S.mt + row * FP) + ks * 64 + (lane2 >> 4) * 16);
        acc1[rt] = __builtin_amdgcn_mfma_f32_16x16x32_bf16(wf3[ks], mf, acc1[rt], 0, 0, 0);
      }
  }
#pragma unroll
  for (int rt = 0; rt < 3; ++rt) {
    int row = rt * 16 + (lane2 & 15);
    if (row < nvalid) {
      int col = w2 * 16 + (lane2 >> 4) * 4;
      uint2 hv = *(const uint2*)(S.ht + row * HP + col);
      float o0 = __uint_as_float(hv.x << 16) + silu_f(acc1[rt][0]);
      float o1 = __uint_as_float(hv.x & 0xffff0000u) + silu_f(acc1[rt][1]);
      float o2 = __uint_as_float(hv.y << 16) + silu_f(acc1[rt][2]);
      float o3 = __uint_as_float(hv.y & 0xffff0000u) + silu_f(acc1[rt][3]);
      *(uint2*)(out_h + (size_t)(node0 + row) * D + col) = make_uint2(pack2(o0, o1), pack2(o2, o3));
    }
  }
  if (tid2 < nvalid) {
    int nd = node0 + tid2;
    out_pos[nd * 3 + 0] = f2bf(bf2f(pos[nd * 3 + 0]) + S.cacc[tid2 * 4 + 0]);
    out_pos[nd * 3 + 1] = f2bf(bf2f(pos[nd * 3 + 1]) + S.cacc[tid2 * 4 + 1]);
    out_pos[nd * 3 + 2] = f2bf(bf2f(pos[nd * 3 + 2]) + S.cacc[tid2 * 4 + 2]);
  }
}
}

extern "C" void kernel_launch(void* const* d_in, const int* in_sizes, int n_in, void* d_out, int out_size, void* d_ws,
                              size_t ws_size, hipStream_t stream) {
  const u16* h = (const u16*)d_in[0];
  const u16* pos = (const u16*)d_in[1];
  const u16* eattr = (const u16*)d_in[2];
  const int* src = (const int*)d_in[3];
  const int* dst = (const int*)d_in[4];
  const u16* w_e = (const u16*)d_in[5];
  const u16* w_x = (const u16*)d_in[6];
  const u16* w_h = (const u16*)d_in[7];
  const int N = in_sizes[0] / D;
  const int E = in_sizes[3];
  u16* out_h = (u16*)d_out;
  u16* out_pos = out_h + (size_t)N * D;

  const int NBINS = (N + TILE - 1) / TILE;
  const int C = MAXC;
  const int CH = (E + C - 1) / C;
  char* ws = (char*)d_ws;
  size_t o = 0;
  auto take = [&](size_t bytes) { char* p = ws + o; o += (bytes + 255) & ~(size_t)255; return p; };
  u16* Q = (u16*)take((size_t)N * D * 2);
  uint4* rec = (uint4*)take((size_t)C * CH * 16);
  int* off = (int*)take((size_t)(NBINS + 1) * C * 4);
  u16* WT = (u16*)take((size_t)3 * 8192 * 16);

  const int NG = ((N + 63) / 64) * 4;
  const int NT = 24;
  const size_t lds1 = 32768 > (size_t)(NBINS + 8) * 4 ? 32768 : (size_t)(NBINS + 8) * 4;
  k1_kernel<<<C + NG + NT, 256, lds1, stream>>>(h, w_e, w_h, src, dst, eattr, pos, Q, rec, off, WT, N, E, C, CH, NBINS, NG);
  hipFuncSetAttribute((const void*)k2_kernel, hipFuncAttributeMaxDynamicSharedMemorySize, (int)sizeof(K2Lds));
  k2_kernel<<<NBINS, 1024, sizeof(K2Lds), stream>>>(h, pos, w_e, w_x, Q, rec, off, WT, out_h, out_pos, N, C, CH);
}
```

```cpp
#include <hip/hip_runtime.h>
#include <hip/hip_bf16.h>
#include <stdint.h>

typedef unsigned short u16;
typedef __attribute__((ext_vector_type(8))) short bf16x8;
typedef __attribute__((ext_vector_type(4))) short s16x4;
typedef __attribute__((ext_vector_type(4))) float f32x4;
typedef __attribute__((ext_vector_type(2))) float f32x2;

constexpr int D = 256;
constexpr int TILE = 40;
constexpr int TROWS = 48;
constexpr int MAXC = 256;
constexpr int MAXE = 1024;
constexpr int HP = 264;
constexpr int FP = 260;
#define NLOG2E (-1.4426950408889634f)
#define NLN2 (-0.6931471805599453f)

__device__ __forceinline__ float bf2f(u16 x) { return __uint_as_float(((unsigned)x) << 16); }
__device__ __forceinline__ u16 f2bf(float f) { return __builtin_bit_cast(u16, (__bf16)f); }
__device__ __forceinline__ unsigned pack2(float lo, float hi) { return (unsigned)f2bf(lo) | ((unsigned)f2bf(hi) << 16); }
__device__ __forceinline__ float rbf(float f) { return bf2f(f2bf(f)); }
__device__ __forceinline__ float silu_f(float x) {
  float t = __builtin_amdgcn_exp2f(NLOG2E * x);
  return x * __builtin_amdgcn_rcpf(1.0f + t);
}
__device__ __forceinline__ unsigned off_b(unsigned row, unsigned ch) {
  return 128u * row + 16u * (ch ^ ((((row >> 1) & 1) | (((row >> 3) & 1) << 1)) << 1));
}
__device__ __forceinline__ uint4 make_rec(unsigned d, unsigned s, unsigned a, const u16* ps, const u16* pd) {
  unsigned b = d / TILE;
  float dx = rbf(bf2f(pd[0]) - bf2f(ps[0])), dy = rbf(bf2f(pd[1]) - bf2f(ps[1])), dz = rbf(bf2f(pd[2]) - bf2f(ps[2]));
  float d2 = rbf(dx * dx) + rbf(dy * dy) + rbf(dz * dz);
  return make_uint4(s, (d - b * TILE) | (a << 16), (unsigned)f2bf(dx) | ((unsigned)f2bf(dy) << 16),
                    (unsigned)f2bf(dz) | ((unsigned)f2bf(d2) << 16));
}

__global__ __launch_bounds__(256) void k1_kernel(const u16* __restrict__ h, const u16* __restrict__ w_e,
                                                 const u16* __restrict__ w_h, const int* __restrict__ src,
                                                 const int* __restrict__ dst, const u16* __restrict__ eattr,
                                                 const u16* __restrict__ pos, u16* __restrict__ Q, uint4* __restrict__ rec,
                                                 int* __restrict__ off, u16* __restrict__ WT, int N, int E, int C, int CH,
                                                 int NBINS, int NG) {
  extern __shared__ __attribute__((aligned(16))) char smem[];
  const int tid = threadIdx.x, lane = tid & 63, w = tid >> 6;
  const int bid = blockIdx.x;
  if (bid < C) {
    int* hist = (int*)smem;
    int* wsum = hist + NBINS + 1;
    const int c = bid;
    const int e0 = c * CH, e1 = min(E, e0 + CH);
    for (int i = tid; i <= NBINS; i += 256) hist[i] = 0;
    __syncthreads();
    unsigned dv0[10], sv0[10], av0[10];
    u16 ps0[10][3], pd0[10][3];
#pragma unroll
    for (int i = 0; i < 10; ++i) {
      int e = e0 + tid + 256 * i;
      bool ok = e < e1;
      dv0[i] = ok ? (unsigned)dst[e] : 0xffffffffu; sv0[i] = ok ? (unsigned)src[e] : 0u; av0[i] = ok ? (unsigned)eattr[e] : 0u;
    }
#pragma unroll
    for (int i = 0; i < 10; ++i) {
      unsigned d = dv0[i] == 0xffffffffu ? 0u : dv0[i];
#pragma unroll
      for (int k = 0; k < 3; ++k) { ps0[i][k] = pos[sv0[i] * 3 + k]; pd0[i][k] = pos[d * 3 + k]; }
    }
#pragma unroll
    for (int i = 0; i < 10; ++i) if (dv0[i] != 0xffffffffu) atomicAdd(&hist[dv0[i] / TILE], 1);
    for (int eb = e0 + 2560; eb < e1; eb += 2560) {
      unsigned dv[10];
#pragma unroll
      for (int i = 0; i < 10; ++i) { int e = eb + tid + 256 * i; dv[i] = e < e1 ? (unsigned)dst[e] : 0xffffffffu; }
#pragma unroll
      for (int i = 0; i < 10; ++i) if (dv[i] != 0xffffffffu) atomicAdd(&hist[dv[i] / TILE], 1);
    }
    __syncthreads();
    const int per = (NBINS + 255) / 256;
    int s = 0;
    for (int i = 0; i < per; ++i) { int b = tid * per + i; if (b < NBINS) s += hist[b]; }
    int inc = s;
    for (int o = 1; o < 64; o <<= 1) { int t = __shfl_up(inc, o, 64); if (lane >= o) inc += t; }
    if (lane == 63) wsum[w] = inc;
    __syncthreads();
    int base = inc - s;
    for (int i = 0; i < w; ++i) base += wsum[i];
    for (int i = 0; i < per; ++i) {
      int b = tid * per + i;
      if (b < NBINS) { int t = hist[b]; hist[b] = base; off[b * C + c] = base; base += t; }
    }
    if (tid == 255) off[NBINS * C + c] = base;
    __syncthreads();
    uint4* out = rec + e0;
#pragma unroll
    for (int i = 0; i < 10; ++i) if (dv0[i] != 0xffffffffu) {
      int p = atomicAdd(&hist[dv0[i] / TILE], 1);
      out[p] = make_rec(dv0[i], sv0[i], av0[i], ps0[i], pd0[i]);
    }
    for (int eb = e0 + 2560; eb < e1; eb += 2560) {
      unsigned dv[10], sv[10], av[10];
#pragma unroll
      for (int i = 0; i < 10; ++i) {
        int e = eb + tid + 256 * i;
        bool ok = e < e1;
        dv[i] = ok ? (unsigned)dst[e] : 0xffffffffu; sv[i] = ok ? (unsigned)src[e] : 0u; av[i] = ok ? (unsigned)eattr[e] : 0u;
      }
      u16 ps[10][3], pd[10][3];
#pragma unroll
      for (int i = 0; i < 10; ++i) {
        unsigned d = dv[i] == 0xffffffffu ? 0u : dv[i];
#pragma unroll
        for (int k = 0; k < 3; ++k) { ps[i][k] = pos[sv[i] * 3 + k]; pd[i][k] = pos[d * 3 + k]; }
      }
#pragma unroll
      for (int i = 0; i < 10; ++i) if (dv[i] != 0xffffffffu) {
        int p = atomicAdd(&hist[dv[i] / TILE], 1);
        out[p] = make_rec(dv[i], sv[i], av[i], ps[i], pd[i]);
      }
    }
  } else if (bid < C + NG) {
    const int g = bid - C;
    const int rb = g >> 2, cb = g & 3;
    const u16* We2 = w_e + D * D;
#pragma unroll
    for (int i = 0; i < 8; ++i) {
      int n = tid + 256 * i, row = n >> 3, ch = n & 7;
      *(uint4*)(smem + off_b(row, ch)) = *(const uint4*)(We2 + row * D + cb * 64 + ch * 8);
    }
    bf16x8 hf[8];
    const int myrow = rb * 64 + w * 16 + (lane & 15);
    {
      int row = min(myrow, N - 1);
#pragma unroll
      for (int ks = 0; ks < 8; ++ks) hf[ks] = *(const bf16x8*)(h + (size_t)row * D + ks * 32 + (lane >> 4) * 8);
    }
    __syncthreads();
    f32x4 acc[4];
#pragma unroll
    for (int a = 0; a < 4; ++a) acc[a] = (f32x4){0.f, 0.f, 0.f, 0.f};
    const unsigned gq = lane >> 4, q = (lane & 15) >> 2, p = lane & 3;
#pragma unroll
    for (int ks = 0; ks < 8; ++ks) {
#pragma unroll
      for (int c = 0; c < 4; ++c) {
        typedef __attribute__((address_space(3))) s16x4* lp;
        s16x4 lo = __builtin_amdgcn_ds_read_tr16_b64_v4i16((lp)(smem + off_b(32 * ks + 8 * gq + q, 2 * c + (p >> 1)) + 8 * (p & 1)));
        s16x4 hi = __builtin_amdgcn_ds_read_tr16_b64_v4i16((lp)(smem + off_b(32 * ks + 8 * gq + 4 + q, 2 * c + (p >> 1)) + 8 * (p & 1)));
        bf16x8 wf = {lo[0], lo[1], lo[2], lo[3], hi[0], hi[1], hi[2], hi[3]};
        acc[c] = __builtin_amdgcn_mfma_f32_16x16x32_bf16(wf, hf[ks], acc[c], 0, 0, 0);
      }
    }
    if (myrow < N) {
#pragma unroll
      for (int c = 0; c < 4; ++c) {
        uint2 v = make_uint2(pack2(acc[c][0] * NLOG2E, acc[c][1] * NLOG2E), pack2(acc[c][2] * NLOG2E, acc[c][3] * NLOG2E));
        *(uint2*)(Q + (size_t)myrow * D + cb * 64 + c * 16 + gq * 4) = v;
      }
    }
  } else {
    const int t0 = (bid - C - NG) * 1024 + tid;
#pragma unroll
    for (int i = 0; i < 4; ++i) {
      int x = t0 + 256 * i;
      if (x < 3 * 8192) {
        int mat = x >> 13, r = x & 8191, ct = r >> 9, ks = (r >> 6) & 7, l = r & 63;
        const u16* W = mat == 0 ? w_e : (mat == 1 ? w_h : w_h + D * D);
        const u16* pw = W + (size_t)(32 * ks + 8 * (l >> 4)) * D + 16 * ct + (l & 15);
        unsigned v[4];
#pragma unroll
        for (int j = 0; j < 4; ++j) v[j] = (unsigned)pw[(2 * j) * D] | ((unsigned)pw[(2 * j + 1) * D] << 16);
        *(uint4*)(WT + (size_t)x * 8) = make_uint4(v[0], v[1], v[2], v[3]);
      }
    }
  }
}

struct K2Lds {
  u16 ht[TROWS * HP];
  float pt[TILE * FP];
  float mt[TILE * FP];
  float4 srt4[MAXE];
  float4 srtd[MAXE];
  unsigned long long key[MAXE];
  int ncnt[TILE + 8];
  int nstart[TILE + 8];
  float cacc[TILE * 4];
  int seg_s[MAXC], seg_b[MAXC + 1];
};

__device__ __forceinline__ void sort_batch(K2Lds& S, const uint4* __restrict__ rec, int CH, int b0, int total, int tid, int lane, int w) {
  const int gp = b0 + tid;
  const bool v = gp < total;
  uint4 r = make_uint4(0, 0, 0, 0);
  if (v) {
    int lo = 0, hi = MAXC - 1;
    while (lo < hi) { int mid = (lo + hi + 1) >> 1; if (S.seg_b[mid] <= gp) lo = mid; else hi = mid - 1; }
    r = rec[(size_t)lo * CH + S.seg_s[lo] + (gp - S.seg_b[lo])];
  }
  const int node = r.y & 0xffff;
  int slot = 0;
  if (v) slot = atomicAdd(&S.ncnt[node], 1);
  __syncthreads();
  if (w == 0) {
    int cnt = lane < TILE ? S.ncnt[lane] : 0;
    int ic = cnt;
    for (int o = 1; o < 64; o <<= 1) { int t = __shfl_up(ic, o, 64); if (lane >= o) ic += t; }
    if (lane <= TILE) S.nstart[lane] = ic - cnt;
  }
  __syncthreads();
  const unsigned long long mykey = ((unsigned long long)r.x << 32) | (r.y >> 16);
  const int k = v ? S.nstart[node] + slot : 0;
  if (v) S.key[k] = mykey;
  __syncthreads();
  if (v) {
    const int kb = S.nstart[node], ke = S.nstart[node + 1];
    int rank = 0;
    for (int j = kb; j < ke; ++j) {
      unsigned long long o = S.key[j];
      rank += (o < mykey || (o == mykey && j < k)) ? 1 : 0;
    }
    S.srt4[kb + rank] = make_float4(__uint_as_float(r.x), __uint_as_float(r.w & 0xffff0000u), __uint_as_float(r.y & 0xffff0000u), 0.f);
    S.srtd[kb + rank] = make_float4(__uint_as_float(r.z << 16), __uint_as_float(r.z & 0xffff0000u), __uint_as_float(r.w << 16), 0.f);
  }
}

__global__ __launch_bounds__(1024) void k2_kernel(const u16* __restrict__ h, const u16* __restrict__ pos,
                                                  const u16* __restrict__ w_e, const u16* __restrict__ w_x,
                                                  const u16* __restrict__ Q, const uint4* __restrict__ rec,
                                                  const int* __restrict__ off, const u16* __restrict__ WT,
                                                  u16* __restrict__ out_h, u16* __restrict__ out_pos, int N, int C, int CH) {
  extern __shared__ __attribute__((aligned(16))) char smem[];
  K2Lds& S = *(K2Lds*)smem;
  const int tid = threadIdx.x, lane = tid & 63, w = tid >> 6;
  const int bin = blockIdx.x;
  const int node0 = bin * TILE;
  const int nvalid = min(TILE, N - node0);
  const bf16x8* WTf = (const bf16x8*)WT;

  if (w == 1) {
    int ss[4], sn[4], tot = 0;
#pragma unroll
    for (int i = 0; i < 4; ++i) {
      int c = lane * 4 + i;
      ss[i] = 0; sn[i] = 0;
      if (c < C) { ss[i] = off[bin * C + c]; sn[i] = off[(bin + 1) * C + c] - ss[i]; }
      tot += sn[i];
    }
    int inc = tot;
    for (int o = 1; o < 64; o <<= 1) { int t = __shfl_up(inc, o, 64); if (lane >= o) inc += t; }
    int b = inc - tot;
#pragma unroll
    for (int i = 0; i < 4; ++i) { S.seg_s[lane * 4 + i] = ss[i]; S.seg_b[lane * 4 + i] = b; b += sn[i]; }
    if (lane == 63) S.seg_b[MAXC] = inc;
  }
#pragma unroll
  for (int i = 0; i < 2; ++i) {
    int n = tid + 1024 * i, row = n >> 5, ch = n & 31;
    if (n < TROWS * 32) {
      int gr = min(node0 + row, N - 1);
      *(uint4*)(S.ht + row * HP + ch * 8) = *(const uint4*)(h + (size_t)gr * D + ch * 8);
    }
  }
  f32x4 acc1[3];
  int total;
  {
    bf16x8 wf1[8], wf2[8];
#pragma unroll
    for (int ks = 0; ks < 8; ++ks) {
      wf1[ks] = WTf[0 * 8192 + (w * 8 + ks) * 64 + lane];
      wf2[ks] = WTf[1 * 8192 + (w * 8 + ks) * 64 + lane];
    }
    if (tid < TILE + 8) S.ncnt[tid] = 0;
    __syncthreads();
    total = S.seg_b[MAXC];
    sort_batch(S, rec, CH, 0, total, tid, lane, w);
    f32x4 accp[3];
#pragma unroll
    for (int rt = 0; rt < 3; ++rt) { accp[rt] = (f32x4){0.f, 0.f, 0.f, 0.f}; acc1[rt] = (f32x4){0.f, 0.f, 0.f, 0.f}; }
#pragma unroll
    for (int ks = 0; ks < 8; ++ks)
#pragma unroll
      for (int rt = 0; rt < 3; ++rt) {
        bf16x8 hf = *(const bf16x8*)(S.ht + (rt * 16 + (lane & 15)) * HP + ks * 32 + (lane >> 4) * 8);
        accp[rt] = __builtin_amdgcn_mfma_f32_16x16x32_bf16(wf1[ks], hf, accp[rt], 0, 0, 0);
        acc1[rt] = __builtin_amdgcn_mfma_f32_16x16x32_bf16(wf2[ks], hf, acc1[rt], 0, 0, 0);
      }
#pragma unroll
    for (int rt = 0; rt < 3; ++rt) {
      int row = rt * 16 + (lane & 15);
      if (row < TILE) *(f32x4*)(S.pt + row * FP + w * 16 + (lane >> 4) * 4) = accp[rt] * NLOG2E;
    }
    __syncthreads();
  }

  {
    f32x2 wd[2], wa[2], wx[2];
    {
      uint2 a = *(const uint2*)(w_e + 2 * D * D + lane * 4);
      uint2 b = *(const uint2*)(w_e + (2 * D + 1) * D + lane * 4);
      uint2 c = *(const uint2*)(w_x + lane * 4);
      wd[0] = (f32x2){__uint_as_float(a.x << 16) * NLOG2E, __uint_as_float(a.x & 0xffff0000u) * NLOG2E};
      wd[1] = (f32x2){__uint_as_float(a.y << 16) * NLOG2E, __uint_as_float(a.y & 0xffff0000u) * NLOG2E};
      wa[0] = (f32x2){__uint_as_float(b.x << 16) * NLOG2E, __uint_as_float(b.x & 0xffff0000u) * NLOG2E};
      wa[1] = (f32x2){__uint_as_float(b.y << 16) * NLOG2E, __uint_as_float(b.y & 0xffff0000u) * NLOG2E};
      wx[0] = (f32x2){__uint_as_float(c.x << 16), __uint_as_float(c.x & 0xffff0000u)};
      wx[1] = (f32x2){__uint_as_float(c.y << 16), __uint_as_float(c.y & 0xffff0000u)};
    }
    for (int b0 = 0;;) {
      for (int nl = w; nl < nvalid; nl += 16) {
        const int k_beg = S.nstart[nl], k_end = S.nstart[nl + 1];
        f32x2 p[2], am[2];
        float cx = 0.f, cy = 0.f, cz = 0.f;
        {
          float4 a = *(const float4*)(S.pt + nl * FP + lane * 4);
          p[0] = (f32x2){a.x, a.y}; p[1] = (f32x2){a.z, a.w};
        }
        am[0] = (f32x2){0.f, 0.f}; am[1] = (f32x2){0.f, 0.f};
        for (int k0 = k_beg; k0 < k_end; k0 += 16) {
          uint2 q[16];
#pragma unroll
          for (int u = 0; u < 16; ++u) {
            if (k0 + u < k_end) q[u] = *(const uint2*)((const char*)Q + ((size_t)__float_as_uint(S.srt4[k0 + u].x) * (D * 2) + lane * 8));
          }
          __builtin_amdgcn_sched_barrier(0);
#pragma unroll
          for (int u = 0; u < 16; ++u) {
            if (k0 + u < k_end) {
              float4 s = S.srt4[k0 + u];
              float4 dd = S.srtd[k0 + u];
              f32x2 sc2 = {0.f, 0.f};
#pragma unroll
              for (int j = 0; j < 2; ++j) {
                unsigned qq = j ? q[u].y : q[u].x;
                f32x2 qv = {__uint_as_float(qq << 16), __uint_as_float(qq & 0xffff0000u)};
                f32x2 x = __builtin_elementwise_fma(wa[j], (f32x2){s.z, s.z}, __builtin_elementwise_fma(wd[j], (f32x2){s.y, s.y}, p[j])) + qv;
                f32x2 t = {__builtin_amdgcn_exp2f(x.x), __builtin_amdgcn_exp2f(x.y)};
                t = t + (f32x2){1.f, 1.f};
                f32x2 rr = {__builtin_amdgcn_rcpf(t.x), __builtin_amdgcn_rcpf(t.y)};
                f32x2 m = x * rr;
                am[j] += m;
                sc2 = __builtin_elementwise_fma(wx[j], m, sc2);
              }
              float sc = sc2.x + sc2.y;
              cx = fmaf(dd.x, sc, cx); cy = fmaf(dd.y, sc, cy); cz = fmaf(dd.z, sc, cz);
            }
            __builtin_amdgcn_sched_barrier(0);
          }
        }
        for (int o = 32; o >= 1; o >>= 1) {
          cx += __shfl_xor(cx, o, 64); cy += __shfl_xor(cy, o, 64); cz += __shfl_xor(cz, o, 64);
        }
        {
          float4* mp = (float4*)(S.mt + nl * FP + lane * 4);
          float4 a = make_float4(am[0].x * NLN2, am[0].y * NLN2, am[1].x * NLN2, am[1].y * NLN2);
          if (b0 > 0) { float4 oa = *mp; a.x += oa.x; a.y += oa.y; a.z += oa.z; a.w += oa.w; }
          if (b0 + MAXE < total) *mp = a;
          else
            *(uint2*)((char*)(S.mt + nl * FP) + lane * 8) = make_uint2(pack2(a.x, a.y), pack2(a.z, a.w));
          if (lane == 0) {
            cx *= NLN2; cy *= NLN2; cz *= NLN2;
            if (b0 > 0) { cx += S.cacc[nl * 4 + 0]; cy += S.cacc[nl * 4 + 1]; cz += S.cacc[nl * 4 + 2]; }
            S.cacc[nl * 4 + 0] = cx; S.cacc[nl * 4 + 1] = cy; S.cacc[nl * 4 + 2] = cz;
          }
        }
      }
      b0 += MAXE;
      if (b0 >= total) break;
      __syncthreads();
      if (tid < TILE + 8) S.ncnt[tid] = 0;
      __syncthreads();
      sort_batch(S, rec, CH, b0, total, tid, lane, w);
      __syncthreads();
    }
  }

  {
    int tid2 = threadIdx.x;
    asm volatile("" : "+v"(tid2));
    const int lane2 = tid2 & 63, w2 = __builtin_amdgcn_readfirstlane(tid2 >> 6);
    {
      bf16x8 wf3[8];
#pragma unroll
      for (int ks = 0; ks < 8; ++ks) wf3[ks] = WTf[2 * 8192 + (w2 * 8 + ks) * 64 + lane2];
      __syncthreads();
#pragma unroll
      for (int ks = 0; ks < 8; ++ks)
#pragma unroll
        for (int rt = 0; rt < 3; ++rt) {
          int row = min(rt * 16 + (lane2 & 15), TILE - 1);
          bf16x8 mf = *(const bf16x8*)((const char*)(S.mt + row * FP) + ks * 64 + (lane2 >> 4) * 16);
          acc1[rt] = __builtin_amdgcn_mfma_f32_16x16x32_bf16(wf3[ks], mf, acc1[rt], 0, 0, 0);
        }
    }
#pragma unroll
    for (int rt = 0; rt < 3; ++rt) {
      int row = rt * 16 + (lane2 & 15);
      if (row < nvalid) {
        int col = w2 * 16 + (lane2 >> 4) * 4;
        uint2 hv = *(const uint2*)(S.ht + row * HP + col);
        float o0 = __uint_as_float(hv.x << 16) + silu_f(acc1[rt][0]);
        float o1 = __uint_as_float(hv.x & 0xffff0000u) + silu_f(acc1[rt][1]);
        float o2 = __uint_as_float(hv.y << 16) + silu_f(acc1[rt][2]);
        float o3 = __uint_as_float(hv.y & 0xffff0000u) + silu_f(acc1[rt][3]);
        *(uint2*)(out_h + (size_t)(node0 + row) * D + col) = make_uint2(pack2(o0, o1), pack2(o2, o3));
      }
    }
    if (tid2 < nvalid) {
      int nd = node0 + tid2;
      out_pos[nd * 3 + 0] = f2bf(bf2f(pos[nd * 3 + 0]) + S.cacc[tid2 * 4 + 0]);
      out_pos[nd * 3 + 1] = f2bf(bf2f(pos[nd * 3 + 1]) + S.cacc[tid2 * 4 + 1]);
      out_pos[nd * 3 + 2] = f2bf(bf2f(pos[nd * 3 + 2]) + S.cacc[tid2 * 4 + 2]);
    }
  }
}

extern "C" void kernel_launch(void* const* d_in, const int* in_sizes, int n_in, void* d_out, int out_size, void* d_ws,
                              size_t ws_size, hipStream_t stream) {
  const u16* h = (const u16*)d_in[0];
  const u16* pos = (const u16*)d_in[1];
  const u16* eattr = (const u16*)d_in[2];
  const int* src = (const int*)d_in[3];
  const int* dst = (const int*)d_in[4];
  const u16* w_e = (const u16*)d_in[5];
  const u16* w_x = (const u16*)d_in[6];
  const u16* w_h = (const u16*)d_in[7];
  const int N = in_sizes[0] / D;
  const int E = in_sizes[3];
  u16* out_h = (u16*)d_out;
  u16* out_pos = out_h + (size_t)N * D;

  const int NBINS = (N + TILE - 1) / TILE;
  const int C = MAXC;
  const int CH = (E + C - 1) / C;
  char* ws = (char*)d_ws;
  size_t o = 0;
  auto take = [&](size_t bytes) { char* p = ws + o; o += (bytes + 255) & ~(size_t)255; return p; };
  u16* Q = (u16*)take((size_t)N * D * 2);
  uint4* rec = (uint4*)take((size_t)C * CH * 16);
  int* off = (int*)take((size_t)(NBINS + 1) * C * 4);
  u16* WT = (u16*)take((size_t)3 * 8192 * 16);

  const int NG = ((N + 63) / 64) * 4;
  const int NT = 24;
  const size_t lds1 = 32768 > (size_t)(NBINS + 8) * 4 ? 32768 : (size_t)(NBINS + 8) * 4;
  k1_kernel<<<C + NG + NT, 256, lds1, stream>>>(h, w_e, w_h, src, dst, eattr, pos, Q, rec, off, WT, N, E, C, CH, NBINS, NG);
  (void)hipFuncSetAttribute((const void*)k2_kernel, hipFuncAttributeMaxDynamicSharedMemorySize, (int)sizeof(K2Lds));
  k2_kernel<<<NBINS, 1024, sizeof(K2Lds), stream>>>(h, pos, w_e, w_x, Q, rec, off, WT, out_h, out_pos, N, C, CH);
}
```
